```python
import torch

def _softmax(x, axis=-1):
    return torch.softmax(x, dim=axis)
def _gelu(x):
    return torch.nn.functional.gelu(x, approximate="tanh")
def _silu(x):
    return torch.nn.functional.silu(x)
def _sigmoid(x):
    return torch.sigmoid(x)
def _layernorm(x, axis=-1, eps=1e-5):
    m = x.mean(dim=axis, keepdim=True)
    v = x.var(dim=axis, keepdim=True, unbiased=False)
    return (x - m) / torch.sqrt(v + eps)
def _scatter_add(tgt, idx, src, axis=0):
    return tgt.clone().index_add_(axis, idx, src)
def _segment_sum(src, seg, n):
    out = torch.zeros((n,) + src.shape[1:], dtype=src.dtype)
    return out.index_add_(0, seg, src)
def _pad(x, pw):
    flat = tuple(v for pair in reversed(pw) for v in pair)
    return torch.nn.functional.pad(x, flat)

kernel_name = 'standalone__gqa_block_cached__decode'

def setup_inputs():

    _w = (lambda a: (lambda t: t.to(torch.bfloat16) if t.is_floating_point() else t)(torch.as_tensor(a)))
    B, Sq, D = 2, 1, 576
    Hq, Hkv, Sk, Dh = 32, 16, 32768, 128
    assert Hq % Hkv == 0, (Hq, Hkv)
    return {
        "x": _w((lambda *s: torch.randn(*s, dtype=torch.bfloat16) * 0.8)(B, Sq, D)),
        "k_cache": _w((lambda *s: torch.randn(*s, dtype=torch.bfloat16) * 0.8)(B, Hkv, Sk, Dh)),
        "v_cache": _w((lambda *s: torch.randn(*s, dtype=torch.bfloat16) * 0.05)(B, Hkv, Sk, Dh)),
        "wq": _w((lambda *s: torch.randn(*s, dtype=torch.bfloat16) * 0.8)(D, Hq * Dh) * (D ** -0.5)),
        "wo": _w((lambda *s: torch.randn(*s, dtype=torch.bfloat16) * 0.05)(Hq * Dh, D) * ((Hq * Dh) ** -0.5)),
    }

def reference(x, k_cache, v_cache, wq, wo):
    B, Sq, D = x.shape
    Hq, Dh = 32, 128
    Hkv = k_cache.shape[1]
    rep = Hq // Hkv
    q = (lambda x, *d: x.permute(*d))((x @ wq).reshape(B, Sq, Hq, Dh), 0, 2, 1, 3)
    k = (lambda x, n, axis: torch.repeat_interleave(x, n, dim=axis))(k_cache, rep, axis=1)
    v = (lambda x, n, axis: torch.repeat_interleave(x, n, dim=axis))(v_cache, rep, axis=1)
    scores = torch.einsum("bhqd,bhkd->bhqk", q, k) * (Dh ** -0.5)
    p = _softmax(scores, axis=-1)
    o = torch.einsum("bhqk,bhkd->bhqd", p, v)
    return (lambda x, *d: x.permute(*d))(o, 0, 2, 1, 3).reshape(B, Sq, Hq * Dh) @ wo
```

```cpp
#include <hip/hip_runtime.h>
#include <hip/hip_bf16.h>
#include <stdint.h>

#define KV_NT 1
#define PREF2 1

namespace {
constexpr int NB = 2, DM = 576, HQ = 32, HKV = 16, SK = 32768, DH = 128;
constexpr int NSPLIT = 8, NWG = NB * HKV * NSPLIT, NTHR = 512, WAVES = 8;
constexpr int TOK_WG = SK / NSPLIT;
constexpr int TILE = 32;
constexpr int NIT = TOK_WG / (TILE * WAVES);
constexpr int QCOLS = HQ * DH;
constexpr float C2 = 0.088388347648318440f * 1.4426950408889634f;
constexpr int NRED = 144;
constexpr unsigned SPIN_MAX = 1u << 22;

constexpr size_t WS_EPOCH = 0;
constexpr size_t WS_Q = 4096;
constexpr int PSTR = 136;
constexpr size_t WS_P = WS_Q + (size_t)32 * 128 * 8;
constexpr size_t WS_Y = WS_P + (size_t)32 * 8 * 2 * PSTR * 8;
constexpr size_t WS_END = WS_Y + (size_t)NB * 128 * DM * 8;

constexpr int L_WO = 0;
constexpr int L_QP = L_WO + 9216;
constexpr int L_Q = L_QP + 256;
constexpr int MREC = 132;
constexpr int L_MRG = L_Q + 128;
constexpr int L_O = L_MRG + WAVES * 2 * MREC;
constexpr int L_RED = L_O + 32;
constexpr int L_TAG = L_RED + 64;
constexpr int L_END = L_TAG + 4;
constexpr int LDS_PAD_FLOATS = 24 * 1024;
static_assert(L_END <= LDS_PAD_FLOATS, "lds");

using u32x4 = __attribute__((ext_vector_type(4))) unsigned;
using u32x2 = __attribute__((ext_vector_type(2))) unsigned;
using f32x4 = __attribute__((ext_vector_type(4))) float;
using f32x2 = __attribute__((ext_vector_type(2))) float;
using bf16x2 = __attribute__((ext_vector_type(2))) __bf16;
typedef unsigned long long u64;

__device__ __forceinline__ float dot2(unsigned k, unsigned q, float acc) {
  return __builtin_amdgcn_fdot2_f32_bf16(__builtin_bit_cast(bf16x2, k), __builtin_bit_cast(bf16x2, q), acc, false);
}
__device__ __forceinline__ unsigned cvtpk(float lo, float hi) {
  unsigned r; asm volatile("v_cvt_pk_bf16_f32 %0, %1, %2" : "=v"(r) : "v"(lo), "v"(hi)); return r;
}
template <int CTRL> __device__ __forceinline__ float dpp(float x) {
  return __builtin_bit_cast(float, __builtin_amdgcn_mov_dpp(__builtin_bit_cast(int, x), CTRL, 0xf, 0xf, true));
}
constexpr int XOR1 = 0xB1, XOR2 = 0x4E, XOR7 = 0x141, XOR8 = 0x128, ROR4 = 0x124, BC0 = 0x150;
__device__ __forceinline__ float xrow16_max(float x) {
  auto s = __builtin_amdgcn_permlane16_swap(__float_as_uint(x), __float_as_uint(x), false, false);
  x = fmaxf(__uint_as_float(s[0]), __uint_as_float(s[1]));
  auto t = __builtin_amdgcn_permlane32_swap(__float_as_uint(x), __float_as_uint(x), false, false);
  return fmaxf(__uint_as_float(t[0]), __uint_as_float(t[1]));
}
__device__ __forceinline__ float xrow16_sum(float x) {
  auto s = __builtin_amdgcn_permlane16_swap(__float_as_uint(x), __float_as_uint(x), false, false);
  x = __uint_as_float(s[0]) + __uint_as_float(s[1]);
  auto t = __builtin_amdgcn_permlane32_swap(__float_as_uint(x), __float_as_uint(x), false, false);
  return __uint_as_float(t[0]) + __uint_as_float(t[1]);
}
__device__ __forceinline__ float bflo(unsigned v) { return __uint_as_float(v << 16); }
__device__ __forceinline__ float bfhi(unsigned v) { return __uint_as_float(v & 0xffff0000u); }
__device__ __forceinline__ void st_gran(u64* p, unsigned data, unsigned tag) {
  __hip_atomic_store(p, ((u64)tag << 32) | (u64)data, __ATOMIC_RELAXED, __HIP_MEMORY_SCOPE_AGENT);
}
__device__ __forceinline__ u64 ld_gran(const u64* p) {
  return __hip_atomic_load(p, __ATOMIC_RELAXED, __HIP_MEMORY_SCOPE_AGENT);
}
__device__ __forceinline__ void wg_barrier() {
  asm volatile("s_waitcnt lgkmcnt(0)" ::: "memory");
  __builtin_amdgcn_s_barrier();
  asm volatile("" ::: "memory");
}
__device__ __forceinline__ u32x4 ld_kv(const u32x4* p) {
  return __builtin_nontemporal_load(p);
}

struct Tile { u32x4 k[8]; u32x4 v[8]; };

__device__ __forceinline__ void issue(Tile& t, const u32x4* kp, const u32x4* vp) {
#pragma unroll
  for (int i = 0; i < 8; ++i) t.k[i] = ld_kv(kp + i * 64);
#pragma unroll
  for (int i = 0; i < 8; ++i) t.v[i] = ld_kv(vp + i * 64);
}

template <int J> __device__ __forceinline__ float bcast(float p) { return dpp<BC0 + J>(p); }

__device__ __forceinline__ void compute(const Tile& t, const u32x4& q0, const u32x4& q1, int c, float& m, float& l, f32x2 (&acc)[8]) {
  float x[16];
#pragma unroll
  for (int i = 0; i < 8; ++i) {
    float d0 = 0.f, d1 = 0.f;
#pragma unroll
    for (int k = 0; k < 4; ++k) { d0 = dot2(t.k[i][k], q0[k], d0); d1 = dot2(t.k[i][k], q1[k], d1); }
    x[2 * i] = d0; x[2 * i + 1] = d1;
  }
#define TR_STEP(HALF, CTRL, BIT) _Pragma("unroll") for (int h = 0; h < (HALF); ++h) { \
    const float lo_ = x[h], hi_ = x[h + (HALF)]; const float t1_ = lo_ + dpp<CTRL>(lo_), t2_ = hi_ + dpp<CTRL>(hi_); \
    x[h] = (c & (BIT)) ? t2_ : t1_; }
  TR_STEP(8, XOR8, 8) TR_STEP(4, XOR7, 4) TR_STEP(2, XOR2, 2) TR_STEP(1, XOR1, 1)
#undef TR_STEP
  const float sc = x[0] * C2;
  float pm = fmaxf(sc, dpp<XOR2>(sc)); pm = fmaxf(pm, dpp<ROR4>(pm)); pm = fmaxf(pm, dpp<XOR8>(pm)); pm = xrow16_max(pm);
  const float mn = fmaxf(m, pm);
  const float alpha = __builtin_amdgcn_exp2f(m - mn), p = __builtin_amdgcn_exp2f(sc - mn);
  l = fmaf(l, alpha, p); m = mn;
  const float a0 = dpp<0xA0>(alpha), a1 = dpp<0xF5>(alpha);
#pragma unroll
  for (int k = 0; k < 4; ++k) { acc[k] *= a0; acc[4 + k] *= a1; }
  float pb[16];
  pb[0] = bcast<0>(p); pb[1] = bcast<1>(p); pb[2] = bcast<2>(p); pb[3] = bcast<3>(p);
  pb[4] = bcast<4>(p); pb[5] = bcast<5>(p); pb[6] = bcast<6>(p); pb[7] = bcast<7>(p);
  pb[8] = bcast<8>(p); pb[9] = bcast<9>(p); pb[10] = bcast<10>(p); pb[11] = bcast<11>(p);
  pb[12] = bcast<12>(p); pb[13] = bcast<13>(p); pb[14] = bcast<14>(p); pb[15] = bcast<15>(p);
#pragma unroll
  for (int i = 0; i < 8; ++i) {
#pragma unroll
    for (int k = 0; k < 4; ++k) {
      const f32x2 vf = {bflo(t.v[i][k]), bfhi(t.v[i][k])};
      const f32x2 p0 = {pb[2 * i], pb[2 * i]}, p1 = {pb[2 * i + 1], pb[2 * i + 1]};
      acc[k] = __builtin_elementwise_fma(p0, vf, acc[k]);
      acc[4 + k] = __builtin_elementwise_fma(p1, vf, acc[4 + k]);
    }
  }
}

#define SB() __builtin_amdgcn_sched_barrier(0)
template <bool W0>
__device__ __forceinline__ unsigned stream_body(const unsigned short* __restrict__ x, const u32x4* __restrict__ kc, const u32x4* __restrict__ vc,
                                                const unsigned short* __restrict__ wq, const u32x4* __restrict__ wo, u64* qg, unsigned* ep,
                                                float* lds, int lane, int wave, int b, int kvh, int s, int group,
                                                float& m, float& l, f32x2 (&acc)[8]) {
  const int c = lane & 15;
  unsigned tag = 0;
  if (W0) tag = __hip_atomic_load(ep, __ATOMIC_RELAXED, __HIP_MEMORY_SCOPE_AGENT) + 1u;
  const int rg = lane >> 3, cq = lane & 7;
  u32x2 wqv[9]; unsigned short xv[9];
#pragma unroll
  for (int i = 0; i < 9; ++i) {
    const int k = 72 * wave + 8 * i + rg;
    wqv[i] = *reinterpret_cast<const u32x2*>(wq + (size_t)k * QCOLS + kvh * 256 + 32 * s + 4 * cq);
    xv[i] = x[b * DM + k];
  }
  u32x4 wov[5];
  const u32x4* wop = wo + (size_t)(kvh * 256 + 32 * s) * (DM / 8);
#pragma unroll
  for (int i = 0; i < 5; ++i) { const int ch = (i < 4 || wave < 4) ? wave + 8 * i : wave; wov[i] = wop[ch * 64 + lane]; }
  SB();
  const size_t tile0 = ((size_t)group * SK + (size_t)s * TOK_WG + (size_t)wave * TILE) * (DH / 8) + lane;
  const u32x4* kp = kc + tile0;
  const u32x4* vp = vc + tile0;
  constexpr int TSTEP = WAVES * TILE * (DH / 8);
  Tile A, Bt;
  if (!W0) { issue(A, kp, vp); SB(); }
  {
#pragma unroll
    for (int i = 0; i < 9; ++i) asm volatile("" : "+v"(wqv[i]));
    float a0 = 0.f, a1 = 0.f, a2 = 0.f, a3 = 0.f;
#pragma unroll
    for (int i = 0; i < 9; ++i) {
      const float xf = __uint_as_float((unsigned)xv[i] << 16);
      a0 = fmaf(xf, bflo(wqv[i][0]), a0); a1 = fmaf(xf, bfhi(wqv[i][0]), a1);
      a2 = fmaf(xf, bflo(wqv[i][1]), a2); a3 = fmaf(xf, bfhi(wqv[i][1]), a3);
    }
    a0 += dpp<XOR8>(a0); a1 += dpp<XOR8>(a1); a2 += dpp<XOR8>(a2); a3 += dpp<XOR8>(a3);
    a0 = xrow16_sum(a0); a1 = xrow16_sum(a1); a2 = xrow16_sum(a2); a3 = xrow16_sum(a3);
    if (lane < 8) *reinterpret_cast<f32x4*>(lds + L_QP + wave * 32 + 4 * lane) = f32x4{a0, a1, a2, a3};
  }
  wg_barrier();
  if (W0) {
    if (lane < 16) {
      float q0 = 0.f, q1 = 0.f;
#pragma unroll
      for (int w = 0; w < WAVES; ++w) { const f32x2 v = *reinterpret_cast<const f32x2*>(lds + L_QP + w * 32 + 2 * lane); q0 += v[0]; q1 += v[1]; }
      st_gran(qg + 16 * s + lane, cvtpk(q0, q1), tag);
    }
    if (lane == 0) reinterpret_cast<unsigned*>(lds)[L_TAG] = tag;
    u64 g0, g1;
#pragma unroll 1
    for (unsigned it = 0; it < SPIN_MAX; ++it) {
      g0 = ld_gran(qg + lane); g1 = ld_gran(qg + 64 + lane);
      const bool ok = (unsigned)(g0 >> 32) == tag && (unsigned)(g1 >> 32) == tag;
      if (__all(ok)) break;
      __builtin_amdgcn_s_sleep(1);
    }
    reinterpret_cast<unsigned*>(lds)[L_Q + lane] = (unsigned)g0;
    reinterpret_cast<unsigned*>(lds)[L_Q + 64 + lane] = (unsigned)g1;
    SB();
    issue(A, kp, vp);
    SB();
  }
#pragma unroll
  for (int i = 0; i < 5; ++i) { const int ch = (i < 4 || wave < 4) ? wave + 8 * i : wave;
    *reinterpret_cast<u32x4*>(lds + L_WO + ch * 256 + lane * 4) = wov[i]; }
  SB();
  issue(Bt, kp + TSTEP, vp + TSTEP);
  SB();
  wg_barrier();
  const u32x4 q0 = *reinterpret_cast<const u32x4*>(lds + L_Q + 4 * c);
  const u32x4 q1 = *reinterpret_cast<const u32x4*>(lds + L_Q + 64 + 4 * c);
  tag = reinterpret_cast<const unsigned*>(lds)[L_TAG];
  SB();
#pragma unroll 1
  for (int it = 0; it < NIT - 2; it += 2) {
    compute(A, q0, q1, c, m, l, acc);
    SB();
    issue(A, kp + (size_t)(it + 2) * TSTEP, vp + (size_t)(it + 2) * TSTEP);
    SB();
    compute(Bt, q0, q1, c, m, l, acc);
    SB();
    issue(Bt, kp + (size_t)(it + 3) * TSTEP, vp + (size_t)(it + 3) * TSTEP);
    SB();
  }
  compute(A, q0, q1, c, m, l, acc);
  SB();
  compute(Bt, q0, q1, c, m, l, acc);
  return tag;
}

__global__ void __launch_bounds__(NTHR, 2)
gqa_decode_kernel(const unsigned short* __restrict__ x, const u32x4* __restrict__ kc, const u32x4* __restrict__ vc,
                  const unsigned short* __restrict__ wq, const u32x4* __restrict__ wo, unsigned short* __restrict__ out,
                  unsigned char* __restrict__ ws) {
  __shared__ __attribute__((aligned(16))) float lds[LDS_PAD_FLOATS];
  const int tid = threadIdx.x, lane = tid & 63, wave = __builtin_amdgcn_readfirstlane(tid >> 6), c = lane & 15, r = lane >> 4;
  const int bid = blockIdx.x;
  const int j = bid >> 3, kvh = (bid & 7) + 8 * (j & 1), b = (j >> 1) & 1, s = j >> 2;
  const int group = b * HKV + kvh;
  u64* const qg = reinterpret_cast<u64*>(ws + WS_Q) + group * 128;
  u64* const pg = reinterpret_cast<u64*>(ws + WS_P) + (size_t)group * 8 * 2 * PSTR;
  u64* const yg = reinterpret_cast<u64*>(ws + WS_Y);
  unsigned* const ep = reinterpret_cast<unsigned*>(ws + WS_EPOCH);

  float m = -INFINITY, l = 0.f;
  f32x2 acc[8];
#pragma unroll
  for (int k = 0; k < 8; ++k) acc[k] = f32x2{0.f, 0.f};
  unsigned tag;
  if (wave == 0) tag = stream_body<true>(x, kc, vc, wq, wo, qg, ep, lds, lane, wave, b, kvh, s, group, m, l, acc);
  else tag = stream_body<false>(x, kc, vc, wq, wo, qg, ep, lds, lane, wave, b, kvh, s, group, m, l, acc);

#pragma unroll
  for (int k = 0; k < 8; ++k) { acc[k][0] = xrow16_sum(acc[k][0]); acc[k][1] = xrow16_sum(acc[k][1]); }
  l += dpp<XOR2>(l); l += dpp<ROR4>(l); l += dpp<XOR8>(l); l = xrow16_sum(l);
  if (r == 0) {
    float* rec = lds + L_MRG + wave * 2 * MREC;
    *reinterpret_cast<f32x4*>(rec + 4 + 8 * c) = f32x4{acc[0][0], acc[0][1], acc[1][0], acc[1][1]};
    *reinterpret_cast<f32x4*>(rec + 8 + 8 * c) = f32x4{acc[2][0], acc[2][1], acc[3][0], acc[3][1]};
    *reinterpret_cast<f32x4*>(rec + MREC + 4 + 8 * c) = f32x4{acc[4][0], acc[4][1], acc[5][0], acc[5][1]};
    *reinterpret_cast<f32x4*>(rec + MREC + 8 + 8 * c) = f32x4{acc[6][0], acc[6][1], acc[7][0], acc[7][1]};
    if (c < 2) { rec[c * MREC] = m; rec[c * MREC + 1] = l; }
  }
  wg_barrier();
  if (tid < 260) {
    const int g = tid < 256 ? tid >> 7 : (tid >> 1) & 1, d = tid & 127;
    const float* rec = lds + L_MRG + g * MREC;
    float M = -INFINITY;
#pragma unroll
    for (int w = 0; w < WAVES; ++w) M = fmaxf(M, rec[w * 2 * MREC]);
    float o = 0.f;
    const int idx = tid < 256 ? 4 + d : 1;
#pragma unroll
    for (int w = 0; w < WAVES; ++w) o = fmaf(__builtin_amdgcn_exp2f(rec[w * 2 * MREC] - M), rec[w * 2 * MREC + idx], o);
    u64* dst = pg + (s * 2 + g) * PSTR;
    if (tid < 256) st_gran(dst + d, __float_as_uint(o), tag);
    else st_gran(dst + 128 + (tid & 1), __float_as_uint((tid & 1) ? o : M), tag);
  }
  if (wave == 0) {
    const int g = s >> 2, dq = (s & 3) * 32, h = lane >> 5, dd = lane & 31;
    const u64* src = pg + g * PSTR + (size_t)(4 * h) * 2 * PSTR + dq + dd;
    const u64* msrc = pg + g * PSTR + (size_t)((lane & 15) >> 1) * 2 * PSTR + 128 + (lane & 1);
    u64 v0, v1, v2, v3, ml;
#pragma unroll 1
    for (unsigned it = 0; it < SPIN_MAX; ++it) {
      v0 = ld_gran(src); v1 = ld_gran(src + 2 * PSTR); v2 = ld_gran(src + 4 * PSTR); v3 = ld_gran(src + 6 * PSTR); ml = ld_gran(msrc);
      const bool ok = (unsigned)(v0 >> 32) == tag && (unsigned)(v1 >> 32) == tag && (unsigned)(v2 >> 32) == tag &&
                      (unsigned)(v3 >> 32) == tag && (unsigned)(ml >> 32) == tag;
      if (__all(ok)) break;
      __builtin_amdgcn_s_sleep(2);
    }
    const float mlv = __uint_as_float((unsigned)ml);
    const float Ms = dpp<0xA0>(mlv), Ls = dpp<0xF5>(mlv);
    float Mx = fmaxf(Ms, dpp<XOR2>(Ms)); Mx = fmaxf(Mx, dpp<ROR4>(Mx)); Mx = fmaxf(Mx, dpp<XOR8>(Mx));
    const float wgt = __builtin_amdgcn_exp2f(Ms - Mx);
    float Lt = wgt * Ls; Lt += dpp<XOR2>(Lt); Lt += dpp<ROR4>(Lt); Lt += dpp<XOR8>(Lt);
    const float w0 = __shfl(wgt, 8 * h + 0), w1 = __shfl(wgt, 8 * h + 2), w2 = __shfl(wgt, 8 * h + 4), w3 = __shfl(wgt, 8 * h + 6);
    float o = w0 * __uint_as_float((unsigned)v0);
    o = fmaf(w1, __uint_as_float((unsigned)v1), o); o = fmaf(w2, __uint_as_float((unsigned)v2), o); o = fmaf(w3, __uint_as_float((unsigned)v3), o);
    o += __shfl_xor(o, 32);
    o = o / Lt;
    if (lane < 32) lds[L_O + lane] = bflo(cvtpk(o, o) & 0xffffu);
  }
  wg_barrier();
  if (tid < DM / 2) {
    float y0 = 0.f, y1 = 0.f;
    const unsigned* wl = reinterpret_cast<const unsigned*>(lds) + L_WO + tid;
#pragma unroll
    for (int q4 = 0; q4 < 8; ++q4) {
      const f32x4 ov = *reinterpret_cast<const f32x4*>(lds + L_O + 4 * q4);
#pragma unroll
      for (int e = 0; e < 4; ++e) { const unsigned wv = wl[(4 * q4 + e) * (DM / 2)]; y0 = fmaf(ov[e], bflo(wv), y0); y1 = fmaf(ov[e], bfhi(wv), y1); }
    }
    u64* dst = yg + ((size_t)b * 128 + kvh * 8 + s) * DM + 2 * tid;
    st_gran(dst, __float_as_uint(y0), tag);
    st_gran(dst + 1, __float_as_uint(y1), tag);
  }
  if (bid < NRED) {
    const int p = tid >> 2, cc = tid & 3;
    const u64* s0 = yg + (size_t)p * DM + 4 * bid + cc;
    const u64* s1 = s0 + (size_t)128 * DM;
    u64 y0, y1;
#pragma unroll 1
    for (unsigned it = 0; it < SPIN_MAX; ++it) {
      y0 = ld_gran(s0); y1 = ld_gran(s1);
      const bool ok = (unsigned)(y0 >> 32) == tag && (unsigned)(y1 >> 32) == tag;
      if (__all(ok)) break;
      __builtin_amdgcn_s_sleep(4);
    }
    float f0 = __uint_as_float((unsigned)y0), f1 = __uint_as_float((unsigned)y1);
    f0 += dpp<ROR4>(f0); f0 += dpp<XOR8>(f0); f0 = xrow16_sum(f0);
    f1 += dpp<ROR4>(f1); f1 += dpp<XOR8>(f1); f1 = xrow16_sum(f1);
    if (lane < 4) { lds[L_RED + wave * 8 + lane] = f0; lds[L_RED + wave * 8 + 4 + lane] = f1; }
    wg_barrier();
    if (tid < 8) {
      float t = 0.f;
#pragma unroll
      for (int w = 0; w < WAVES; ++w) t += lds[L_RED + w * 8 + tid];
      out[(tid >> 2) * DM + 4 * bid + (tid & 3)] = (unsigned short)(cvtpk(t, t) & 0xffffu);
    }
    if (bid == 0 && tid == 0) __hip_atomic_store(ep, tag, __ATOMIC_RELAXED, __HIP_MEMORY_SCOPE_AGENT);
  }
}
}

extern "C" void kernel_launch(void* const* d_in, const int* in_sizes, int n_in, void* d_out, int out_size, void* d_ws, size_t ws_size,
                              hipStream_t stream) {
  (void)in_sizes; (void)n_in; (void)out_size; (void)ws_size;
  gqa_decode_kernel<<<NWG, NTHR, 0, stream>>>((const unsigned short*)d_in[0], (const u32x4*)d_in[1], (const u32x4*)d_in[2],
                                              (const unsigned short*)d_in[3], (const u32x4*)d_in[4], (unsigned short*)d_out,
                                              (unsigned char*)d_ws);
}
```

```cpp
#include <hip/hip_runtime.h>
#include <hip/hip_bf16.h>
#include <stdint.h>

#define KV_NT 1
#define H1 2
#define H2 4

namespace {
constexpr int NB = 2, DM = 576, HQ = 32, HKV = 16, SK = 32768, DH = 128;
constexpr int NSPLIT = 8, NWG = NB * HKV * NSPLIT, NTHR = 512, WAVES = 8;
constexpr int TOK_WG = SK / NSPLIT;
#define NI 4
constexpr int TILE = 4 * NI;
constexpr int TBYTES = TILE * DH * 2;
constexpr int QCOLS = HQ * DH;
constexpr float C2 = 0.088388347648318440f * 1.4426950408889634f;
constexpr int NRED = 144;
constexpr unsigned SPIN_MAX = 1u << 22;

constexpr size_t WS_EPOCH = 0;
constexpr size_t WS_Q = 4096;
constexpr int PSTR = 136;
constexpr size_t WS_P = WS_Q + (size_t)32 * 128 * 8;
constexpr size_t WS_Y = WS_P + (size_t)32 * 8 * 2 * PSTR * 8;
constexpr size_t WS_CNT = WS_Y + (size_t)NB * 128 * DM * 8;
constexpr size_t WS_END = WS_CNT + 32 * 256;
#define NSMALL_TOK 8192
constexpr unsigned NBIG = (SK - NSMALL_TOK) / TILE;
constexpr unsigned NT = NBIG + NSMALL_TOK / (TILE / 2);

constexpr int L_WO = 0;
constexpr int L_QP = L_WO + 9216;
constexpr int L_Q = L_QP + 256;
constexpr int MREC = 132;
constexpr int L_MRG = L_Q + 128;
constexpr int L_O = L_MRG + WAVES * 2 * MREC;
constexpr int L_RED = L_O + 32;
constexpr int L_TAG = L_RED + 64;
constexpr int L_END = L_TAG + 4;
constexpr int LDS_PAD_FLOATS = 24 * 1024;
static_assert(L_END <= LDS_PAD_FLOATS, "lds");

using u32x4 = __attribute__((ext_vector_type(4))) unsigned;
using u32x2 = __attribute__((ext_vector_type(2))) unsigned;
using f32x4 = __attribute__((ext_vector_type(4))) float;
using f32x2 = __attribute__((ext_vector_type(2))) float;
using bf16x2 = __attribute__((ext_vector_type(2))) __bf16;
typedef unsigned long long u64;

__device__ __forceinline__ float dot2(unsigned k, unsigned q, float acc) {
  return __builtin_amdgcn_fdot2_f32_bf16(__builtin_bit_cast(bf16x2, k), __builtin_bit_cast(bf16x2, q), acc, false);
}
__device__ __forceinline__ unsigned cvtpk(float lo, float hi) {
  unsigned r; asm volatile("v_cvt_pk_bf16_f32 %0, %1, %2" : "=v"(r) : "v"(lo), "v"(hi)); return r;
}
template <int CTRL> __device__ __forceinline__ float dpp(float x) {
  return __builtin_bit_cast(float, __builtin_amdgcn_mov_dpp(__builtin_bit_cast(int, x), CTRL, 0xf, 0xf, true));
}
constexpr int XOR1 = 0xB1, XOR2 = 0x4E, XOR7 = 0x141, XOR8 = 0x128, ROR4 = 0x124, BC0 = 0x150;
__device__ __forceinline__ float xrow16_max(float x) {
  auto s = __builtin_amdgcn_permlane16_swap(__float_as_uint(x), __float_as_uint(x), false, false);
  x = fmaxf(__uint_as_float(s[0]), __uint_as_float(s[1]));
  auto t = __builtin_amdgcn_permlane32_swap(__float_as_uint(x), __float_as_uint(x), false, false);
  return fmaxf(__uint_as_float(t[0]), __uint_as_float(t[1]));
}
__device__ __forceinline__ float xrow16_sum(float x) {
  auto s = __builtin_amdgcn_permlane16_swap(__float_as_uint(x), __float_as_uint(x), false, false);
  x = __uint_as_float(s[0]) + __uint_as_float(s[1]);
  auto t = __builtin_amdgcn_permlane32_swap(__float_as_uint(x), __float_as_uint(x), false, false);
  return __uint_as_float(t[0]) + __uint_as_float(t[1]);
}
__device__ __forceinline__ float bflo(unsigned v) { return __uint_as_float(v << 16); }
__device__ __forceinline__ float bfhi(unsigned v) { return __uint_as_float(v & 0xffff0000u); }
__device__ __forceinline__ void st_gran(u64* p, unsigned data, unsigned tag) {
  __hip_atomic_store(p, ((u64)tag << 32) | (u64)data, __ATOMIC_RELAXED, __HIP_MEMORY_SCOPE_AGENT);
}
__device__ __forceinline__ u64 ld_gran(const u64* p) {
  return __hip_atomic_load(p, __ATOMIC_RELAXED, __HIP_MEMORY_SCOPE_AGENT);
}
__device__ __forceinline__ void wg_barrier() {
  asm volatile("s_waitcnt lgkmcnt(0)" ::: "memory");
  __builtin_amdgcn_s_barrier();
  asm volatile("" ::: "memory");
}
typedef __amdgpu_buffer_rsrc_t rsrc_t;
__device__ __forceinline__ u32x4 ld_kv(rsrc_t r, unsigned voff, unsigned soff) {
  return __builtin_amdgcn_raw_buffer_load_b128(r, voff + soff, 0, KV_NT ? 2 : 0);
}
#define STAMP(i) do { } while (0)
struct Tile { u32x4 k[NI]; u32x4 v[NI]; };

__device__ __forceinline__ void issue(Tile& t, rsrc_t kr, rsrc_t vr, unsigned voff, unsigned soff) {
#pragma unroll
  for (int i = 0; i < NI; ++i) t.k[i] = ld_kv(kr, voff + i * 1024, soff);
#pragma unroll
  for (int i = 0; i < NI; ++i) t.v[i] = ld_kv(vr, voff + i * 1024, soff);
}
template <int LO, int HI> __device__ __forceinline__ void issue_units(Tile& a, Tile& b, rsrc_t kr, rsrc_t vr, unsigned voff, unsigned soff) {
#pragma unroll
  for (int i = 0; i < NI; ++i) if (LO <= 0 && 0 < HI) a.k[i] = ld_kv(kr, voff + i * 1024, soff);
#pragma unroll
  for (int i = 0; i < NI; ++i) if (LO <= 1 && 1 < HI) a.v[i] = ld_kv(vr, voff + i * 1024, soff);
#pragma unroll
  for (int i = 0; i < NI; ++i) if (LO <= 2 && 2 < HI) b.k[i] = ld_kv(kr, voff + i * 1024, soff + 64 * TBYTES);
#pragma unroll
  for (int i = 0; i < NI; ++i) if (LO <= 3 && 3 < HI) b.v[i] = ld_kv(vr, voff + i * 1024, soff + 64 * TBYTES);
}
extern "C" __device__ int llvm_raw_atomic_add(int, rsrc_t, int, int, int) __asm("llvm.amdgcn.raw.ptr.buffer.atomic.add.i32");
__device__ __forceinline__ void refill(Tile& t, rsrc_t kr, rsrc_t vr, unsigned voff, unsigned tkt, bool req, unsigned& tk, rsrc_t cr, int lane) {
  tk = (unsigned)llvm_raw_atomic_add(1, cr, req ? lane * 4 : 64, 0, 0);
  const bool big = tkt < NBIG;
  const unsigned lo = voff + (big ? tkt * TBYTES : NBIG * TBYTES + (tkt - NBIG) * (TBYTES / 2));
  const unsigned hi = big ? lo : 0x40000000u;
  __builtin_amdgcn_sched_barrier(0);
#pragma unroll
  for (int i = 0; i < NI; ++i) t.k[i] = ld_kv(kr, (i < NI / 2 ? lo : hi) + i * 1024, 0);
#pragma unroll
  for (int i = 0; i < NI; ++i) t.v[i] = ld_kv(vr, (i < NI / 2 ? lo : hi) + i * 1024, 0);
}

template <int J> __device__ __forceinline__ float bcast(float p) { return dpp<BC0 + J>(p); }

__device__ __forceinline__ void compute(const Tile& t, const u32x4& q0, const u32x4& q1, int c, bool half, float& m, float& l, f32x2 (&acc)[8]) {
  float x[2 * NI];
#pragma unroll
  for (int i = 0; i < NI; ++i) {
    float d0 = 0.f, d1 = 0.f;
#pragma unroll
    for (int k = 0; k < 4; ++k) { d0 = dot2(t.k[i][k], q0[k], d0); d1 = dot2(t.k[i][k], q1[k], d1); }
    x[2 * i] = d0; x[2 * i + 1] = d1;
  }
#define TR_STEP(HALF, CTRL, BIT) _Pragma("unroll") for (int h = 0; h < (HALF); ++h) { \
    const float lo_ = x[h], hi_ = x[h + (HALF)]; const float t1_ = lo_ + dpp<CTRL>(lo_), t2_ = hi_ + dpp<CTRL>(hi_); \
    x[h] = (c & (BIT)) ? t2_ : t1_; }
  TR_STEP(4, XOR8, 8) TR_STEP(2, XOR7, 4) TR_STEP(1, XOR2, 2)
  x[0] += dpp<XOR1>(x[0]);
  constexpr int SH = 1, HX = XOR1, A0 = 0x00, A1 = 0xAA;
#undef TR_STEP
  const float sc = (half && (c & 8)) ? -INFINITY : x[0] * C2;
  float pm = NI == 8 ? fmaxf(sc, dpp<HX>(sc)) : sc;
  pm = fmaxf(pm, dpp<ROR4>(pm)); pm = fmaxf(pm, dpp<XOR8>(pm)); pm = xrow16_max(pm);
  const float mn = fmaxf(m, pm);
  const float alpha = __builtin_amdgcn_exp2f(m - mn), p = __builtin_amdgcn_exp2f(sc - mn);
  l = fmaf(l, alpha, p); m = mn;
  const float a0 = dpp<A0>(alpha), a1 = dpp<A1>(alpha);
#pragma unroll
  for (int k = 0; k < 4; ++k) { acc[k] *= a0; acc[4 + k] *= a1; }
  float pb[16];
  pb[0] = bcast<0 << SH>(p); pb[1] = bcast<1 << SH>(p); pb[2] = bcast<2 << SH>(p); pb[3] = bcast<3 << SH>(p);
  pb[4] = bcast<4 << SH>(p); pb[5] = bcast<5 << SH>(p); pb[6] = bcast<6 << SH>(p); pb[7] = bcast<7 << SH>(p);
#pragma unroll
  for (int i = 0; i < NI; ++i) {
#pragma unroll
    for (int k = 0; k < 4; ++k) {
      const f32x2 vf = {bflo(t.v[i][k]), bfhi(t.v[i][k])};
      const f32x2 p0 = {pb[2 * i], pb[2 * i]}, p1 = {pb[2 * i + 1], pb[2 * i + 1]};
      acc[k] = __builtin_elementwise_fma(p0, vf, acc[k]);
      acc[4 + k] = __builtin_elementwise_fma(p1, vf, acc[4 + k]);
    }
  }
}
__device__ __forceinline__ void touch(const Tile& t) { asm volatile("" : : "v"(t.v[NI - 1])); }

#define SB() __builtin_amdgcn_sched_barrier(0)
template <bool W0>
__device__ __forceinline__ unsigned stream_body(const unsigned short* __restrict__ x, const u32x4* __restrict__ kc, const u32x4* __restrict__ vc,
                                                const unsigned short* __restrict__ wq, const u32x4* __restrict__ wo, u64* qg, unsigned* ep, unsigned* cnt, unsigned char* ws,
                                                float* lds, int lane, int wave, int b, int kvh, int s, int group,
                                                float& m, float& l, f32x2 (&acc)[8]) {
  const int c = lane & 15;
  unsigned tag = 0;
  if (W0) tag = __hip_atomic_load(ep, __ATOMIC_RELAXED, __HIP_MEMORY_SCOPE_AGENT) + 1u;
  const int rg = lane >> 3, cq = lane & 7;
  u32x2 wqv[9]; unsigned short xv[9];
#pragma unroll
  for (int i = 0; i < 9; ++i) xv[i] = x[b * DM + 72 * wave + 8 * i + rg];
#pragma unroll
  for (int i = 0; i < 9; ++i)
    wqv[i] = *reinterpret_cast<const u32x2*>(wq + (size_t)(72 * wave + 8 * i + rg) * QCOLS + kvh * 256 + 32 * s + 4 * cq);
  SB();
  const rsrc_t kr = __builtin_amdgcn_make_buffer_rsrc((void*)(kc + (size_t)group * SK * (DH / 8)), 0, SK * DH * 2, 0x00020000);
  const rsrc_t vr = __builtin_amdgcn_make_buffer_rsrc((void*)(vc + (size_t)group * SK * (DH / 8)), 0, SK * DH * 2, 0x00020000);
  const unsigned voff = lane * 16, soff0 = (s * WAVES + wave) * TBYTES;
  Tile A, Bt;
  if (!W0) { issue_units<0, H1>(A, Bt, kr, vr, voff, soff0); SB(); }
  {
#pragma unroll
    for (int i = 0; i < 9; ++i) asm volatile("" : "+v"(wqv[i]));
    float a0 = 0.f, a1 = 0.f, a2 = 0.f, a3 = 0.f;
#pragma unroll
    for (int i = 0; i < 9; ++i) {
      const float xf = __uint_as_float((unsigned)xv[i] << 16);
      a0 = fmaf(xf, bflo(wqv[i][0]), a0); a1 = fmaf(xf, bfhi(wqv[i][0]), a1);
      a2 = fmaf(xf, bflo(wqv[i][1]), a2); a3 = fmaf(xf, bfhi(wqv[i][1]), a3);
    }
    a0 += dpp<XOR8>(a0); a1 += dpp<XOR8>(a1); a2 += dpp<XOR8>(a2); a3 += dpp<XOR8>(a3);
    a0 = xrow16_sum(a0); a1 = xrow16_sum(a1); a2 = xrow16_sum(a2); a3 = xrow16_sum(a3);
    if (lane < 8) *reinterpret_cast<f32x4*>(lds + L_QP + wave * 32 + 4 * lane) = f32x4{a0, a1, a2, a3};
  }
  wg_barrier();
  if (W0) {
    STAMP(6);
    if (s == 0) {
      if (lane == 0) __hip_atomic_store(cnt, 256u, __ATOMIC_RELAXED, __HIP_MEMORY_SCOPE_AGENT);
      asm volatile("s_waitcnt vmcnt(0)" ::: "memory");
    }
    if (lane < 16) {
      float q0 = 0.f, q1 = 0.f;
#pragma unroll
      for (int w = 0; w < WAVES; ++w) { const f32x2 v = *reinterpret_cast<const f32x2*>(lds + L_QP + w * 32 + 2 * lane); q0 += v[0]; q1 += v[1]; }
      st_gran(qg + 16 * s + lane, cvtpk(q0, q1), tag);
    }
    if (lane == 0) reinterpret_cast<unsigned*>(lds)[L_TAG] = tag;
    u64 g0, g1;
#pragma unroll 1
    for (unsigned it = 0; it < SPIN_MAX; ++it) {
      g0 = ld_gran(qg + lane); g1 = ld_gran(qg + 64 + lane);
      const bool ok = (unsigned)(g0 >> 32) == tag && (unsigned)(g1 >> 32) == tag;
      if (__all(ok)) break;
      __builtin_amdgcn_s_sleep(1);
    }
    reinterpret_cast<unsigned*>(lds)[L_Q + lane] = (unsigned)g0;
    reinterpret_cast<unsigned*>(lds)[L_Q + 64 + lane] = (unsigned)g1;
    STAMP(7);
  }
  if (!W0) { issue_units<H1, H2>(A, Bt, kr, vr, voff, soff0); SB(); }
  wg_barrier();
  if (W0) issue_units<0, 4>(A, Bt, kr, vr, voff, soff0); else issue_units<H2, 4>(A, Bt, kr, vr, voff, soff0);
  SB();
  u32x4 wov[5];
  const u32x4* wop = wo + (size_t)(kvh * 256 + 32 * s) * (DM / 8);
#pragma unroll
  for (int i = 0; i < 5; ++i) { const int ch = (i < 4 || wave < 4) ? wave + 8 * i : wave; wov[i] = wop[ch * 64 + lane]; }
  SB();
  const u32x4 q0 = *reinterpret_cast<const u32x4*>(lds + L_Q + 4 * c);
  const u32x4 q1 = *reinterpret_cast<const u32x4*>(lds + L_Q + 64 + 4 * c);
  tag = reinterpret_cast<const unsigned*>(lds)[L_TAG];
  STAMP(1);
  SB();
  unsigned tka = 128 + s * WAVES + wave, tkb = 192 + s * WAVES + wave;
  const rsrc_t cr = __builtin_amdgcn_make_buffer_rsrc((void*)cnt, 0, 4, 0x00020000);
  bool va = true, vb = true, ha = false, hb = false;
#pragma unroll 1
  do {
    if (va) compute(A, q0, q1, c, ha, m, l, acc); else touch(A);
    SB();
    unsigned t = va ? __builtin_amdgcn_readfirstlane(tka) : NT;
    va = t < NT; ha = t >= NBIG;
    refill(A, kr, vr, voff, t, va, tka, cr, lane);
    SB();
    if (vb) compute(Bt, q0, q1, c, hb, m, l, acc); else touch(Bt);
    SB();
    t = vb ? __builtin_amdgcn_readfirstlane(tkb) : NT;
    vb = t < NT; hb = t >= NBIG;
    refill(Bt, kr, vr, voff, t, vb, tkb, cr, lane);
    SB();
  } while (va || vb);
#pragma unroll
  for (int i = 0; i < 5; ++i) { const int ch = (i < 4 || wave < 4) ? wave + 8 * i : wave;
    *reinterpret_cast<u32x4*>(lds + L_WO + ch * 256 + lane * 4) = wov[i]; }
  return tag;
}

__global__ void __launch_bounds__(NTHR, 2)
gqa_decode_kernel(const unsigned short* __restrict__ x, const u32x4* __restrict__ kc, const u32x4* __restrict__ vc,
                  const unsigned short* __restrict__ wq, const u32x4* __restrict__ wo, unsigned short* __restrict__ out,
                  unsigned char* __restrict__ ws) {
  __shared__ __attribute__((aligned(16))) float lds[LDS_PAD_FLOATS];
  const int tid = threadIdx.x, lane = tid & 63, wave = __builtin_amdgcn_readfirstlane(tid >> 6), c = lane & 15, r = lane >> 4;
  const int bid = blockIdx.x;
  const int s = bid & 7, group = bid >> 3, b = group >> 4, kvh = group & 15;
  unsigned* const cnt = reinterpret_cast<unsigned*>(ws + WS_CNT + group * 256);
  u64* const qg = reinterpret_cast<u64*>(ws + WS_Q) + group * 128;
  u64* const pg = reinterpret_cast<u64*>(ws + WS_P) + (size_t)group * 8 * 2 * PSTR;
  u64* const yg = reinterpret_cast<u64*>(ws + WS_Y);
  unsigned* const ep = reinterpret_cast<unsigned*>(ws + WS_EPOCH);

  STAMP(0);
  float m = -INFINITY, l = 0.f;
  f32x2 acc[8];
#pragma unroll
  for (int k = 0; k < 8; ++k) acc[k] = f32x2{0.f, 0.f};
  unsigned tag;
  if (wave == 0) tag = stream_body<true>(x, kc, vc, wq, wo, qg, ep, cnt, ws, lds, lane, wave, b, kvh, s, group, m, l, acc);
  else tag = stream_body<false>(x, kc, vc, wq, wo, qg, ep, cnt, ws, lds, lane, wave, b, kvh, s, group, m, l, acc);

#pragma unroll
  for (int k = 0; k < 8; ++k) { acc[k][0] = xrow16_sum(acc[k][0]); acc[k][1] = xrow16_sum(acc[k][1]); }
  if (NI == 8) l += dpp<XOR2>(l);
  l += dpp<ROR4>(l); l += dpp<XOR8>(l); l = xrow16_sum(l);
  if (r == 0) {
    float* rec = lds + L_MRG + wave * 2 * MREC;
    *reinterpret_cast<f32x4*>(rec + 4 + 8 * c) = f32x4{acc[0][0], acc[0][1], acc[1][0], acc[1][1]};
    *reinterpret_cast<f32x4*>(rec + 8 + 8 * c) = f32x4{acc[2][0], acc[2][1], acc[3][0], acc[3][1]};
    *reinterpret_cast<f32x4*>(rec + MREC + 4 + 8 * c) = f32x4{acc[4][0], acc[4][1], acc[5][0], acc[5][1]};
    *reinterpret_cast<f32x4*>(rec + MREC + 8 + 8 * c) = f32x4{acc[6][0], acc[6][1], acc[7][0], acc[7][1]};
    if (NI == 8 ? c < 2 : (c == 0 || c == 2)) { const int g = NI == 8 ? c : c >> 1; rec[g * MREC] = m; rec[g * MREC + 1] = l; }
  }
  STAMP(2);
  wg_barrier();
  STAMP(3);
  if (tid < 260) {
    const int g = tid < 256 ? tid >> 7 : (tid >> 1) & 1, d = tid & 127;
    const float* rec = lds + L_MRG + g * MREC;
    float M = -INFINITY;
#pragma unroll
    for (int w = 0; w < WAVES; ++w) M = fmaxf(M, rec[w * 2 * MREC]);
    float o = 0.f;
    const int idx = tid < 256 ? 4 + d : 1;
#pragma unroll
    for (int w = 0; w < WAVES; ++w) o = fmaf(__builtin_amdgcn_exp2f(rec[w * 2 * MREC] - M), rec[w * 2 * MREC + idx], o);
    u64* dst = pg + (s * 2 + g) * PSTR;
    if (tid < 256) st_gran(dst + d, __float_as_uint(o), tag);
    else st_gran(dst + 128 + (tid & 1), __float_as_uint((tid & 1) ? o : M), tag);
  }
  if (wave == 0) {
    const int g = s >> 2, dq = (s & 3) * 32, h = lane >> 5, dd = lane & 31;
    const u64* src = pg + g * PSTR + (size_t)(4 * h) * 2 * PSTR + dq + dd;
    const u64* msrc = pg + g * PSTR + (size_t)((lane & 15) >> 1) * 2 * PSTR + 128 + (lane & 1);
    u64 v0, v1, v2, v3, ml;
#pragma unroll 1
    for (unsigned it = 0; it < SPIN_MAX; ++it) {
      v0 = ld_gran(src); v1 = ld_gran(src + 2 * PSTR); v2 = ld_gran(src + 4 * PSTR); v3 = ld_gran(src + 6 * PSTR); ml = ld_gran(msrc);
      const bool ok = (unsigned)(v0 >> 32) == tag && (unsigned)(v1 >> 32) == tag && (unsigned)(v2 >> 32) == tag &&
                      (unsigned)(v3 >> 32) == tag && (unsigned)(ml >> 32) == tag;
      if (__all(ok)) break;
      __builtin_amdgcn_s_sleep(2);
    }
    const float mlv = __uint_as_float((unsigned)ml);
    const float Ms = dpp<0xA0>(mlv), Ls = dpp<0xF5>(mlv);
    float Mx = fmaxf(Ms, dpp<XOR2>(Ms)); Mx = fmaxf(Mx, dpp<ROR4>(Mx)); Mx = fmaxf(Mx, dpp<XOR8>(Mx));
    const float wgt = __builtin_amdgcn_exp2f(Ms - Mx);
    float Lt = wgt * Ls; Lt += dpp<XOR2>(Lt); Lt += dpp<ROR4>(Lt); Lt += dpp<XOR8>(Lt);
    const float w0 = __shfl(wgt, 8 * h + 0), w1 = __shfl(wgt, 8 * h + 2), w2 = __shfl(wgt, 8 * h + 4), w3 = __shfl(wgt, 8 * h + 6);
    float o = w0 * __uint_as_float((unsigned)v0);
    o = fmaf(w1, __uint_as_float((unsigned)v1), o); o = fmaf(w2, __uint_as_float((unsigned)v2), o); o = fmaf(w3, __uint_as_float((unsigned)v3), o);
    o += __shfl_xor(o, 32);
    o = o / Lt;
    if (lane < 32) lds[L_O + lane] = bflo(cvtpk(o, o) & 0xffffu);
  }
  wg_barrier();
  STAMP(4);
  if (tid < DM / 2) {
    float y0 = 0.f, y1 = 0.f;
    const unsigned* wl = reinterpret_cast<const unsigned*>(lds) + L_WO + tid;
#pragma unroll
    for (int q4 = 0; q4 < 8; ++q4) {
      const f32x4 ov = *reinterpret_cast<const f32x4*>(lds + L_O + 4 * q4);
#pragma unroll
      for (int e = 0; e < 4; ++e) { const unsigned wv = wl[(4 * q4 + e) * (DM / 2)]; y0 = fmaf(ov[e], bflo(wv), y0); y1 = fmaf(ov[e], bfhi(wv), y1); }
    }
    u64* dst = yg + ((size_t)b * 128 + kvh * 8 + s) * DM + 2 * tid;
    st_gran(dst, __float_as_uint(y0), tag);
    st_gran(dst + 1, __float_as_uint(y1), tag);
  }
  if (bid < NRED) {
    const int p = tid >> 2, cc = tid & 3;
    const u64* s0 = yg + (size_t)p * DM + 4 * bid + cc;
    const u64* s1 = s0 + (size_t)128 * DM;
    u64 y0, y1;
#pragma unroll 1
    for (unsigned it = 0; it < SPIN_MAX; ++it) {
      y0 = ld_gran(s0); y1 = ld_gran(s1);
      const bool ok = (unsigned)(y0 >> 32) == tag && (unsigned)(y1 >> 32) == tag;
      if (__all(ok)) break;
      __builtin_amdgcn_s_sleep(4);
    }
    float f0 = __uint_as_float((unsigned)y0), f1 = __uint_as_float((unsigned)y1);
    f0 += dpp<ROR4>(f0); f0 += dpp<XOR8>(f0); f0 = xrow16_sum(f0);
    f1 += dpp<ROR4>(f1); f1 += dpp<XOR8>(f1); f1 = xrow16_sum(f1);
    if (lane < 4) { lds[L_RED + wave * 8 + lane] = f0; lds[L_RED + wave * 8 + 4 + lane] = f1; }
    wg_barrier();
    if (tid < 8) {
      float t = 0.f;
#pragma unroll
      for (int w = 0; w < WAVES; ++w) t += lds[L_RED + w * 8 + tid];
      out[(tid >> 2) * DM + 4 * bid + (tid & 3)] = (unsigned short)(cvtpk(t, t) & 0xffffu);
    }
    if (bid == 0 && tid == 0) __hip_atomic_store(ep, tag, __ATOMIC_RELAXED, __HIP_MEMORY_SCOPE_AGENT);
  }
  STAMP(5);
}
}

extern "C" void kernel_launch(void* const* d_in, const int* in_sizes, int n_in, void* d_out, int out_size, void* d_ws, size_t ws_size,
                              hipStream_t stream) {
  (void)in_sizes; (void)n_in; (void)out_size; (void)ws_size;
  gqa_decode_kernel<<<NWG, NTHR, 0, stream>>>((const unsigned short*)d_in[0], (const u32x4*)d_in[1], (const u32x4*)d_in[2],
                                              (const unsigned short*)d_in[3], (const u32x4*)d_in[4], (unsigned short*)d_out,
                                              (unsigned char*)d_ws);
}
```

```cpp
#include <hip/hip_runtime.h>
#include <hip/hip_bf16.h>
#include <stdint.h>

#define KV_NT 1
#define STATIC_TILES 1
#define H1 2
#define H2 4

namespace {
constexpr int NB = 2, DM = 576, HQ = 32, HKV = 16, SK = 32768, DH = 128;
constexpr int NSPLIT = 8, NWG = NB * HKV * NSPLIT, NTHR = 512, WAVES = 8;
#define NI 8
constexpr int TILE = 4 * NI;
constexpr int TBYTES = TILE * DH * 2;
constexpr int QCOLS = HQ * DH;
constexpr float C2 = 0.088388347648318440f * 1.4426950408889634f;
constexpr int NRED = 144;
constexpr unsigned SPIN_MAX = 1u << 22;

constexpr size_t WS_EPOCH = 0;
constexpr size_t WS_Q = 4096;
constexpr int PSTR = 136;
constexpr size_t WS_P = WS_Q + (size_t)32 * 128 * 8;
constexpr size_t WS_Y = WS_P + (size_t)32 * 8 * 2 * PSTR * 8;
constexpr size_t WS_CNT = WS_Y + (size_t)NB * 128 * DM * 8;
constexpr size_t WS_END = WS_CNT + 32 * 256;
#define NSMALL_TOK 0
constexpr unsigned NBIG = (SK - NSMALL_TOK) / TILE;
constexpr unsigned NT = NBIG + NSMALL_TOK / (TILE / 2);

constexpr int L_WO = 0;
constexpr int L_QP = L_WO + 9216;
constexpr int L_Q = L_QP + 256;
constexpr int MREC = 132;
constexpr int L_MRG = L_Q + 128;
constexpr int L_O = L_MRG + WAVES * 2 * MREC;
constexpr int L_RED = L_O + 32;
constexpr int L_TAG = L_RED + 64;
constexpr int L_END = L_TAG + 4;
constexpr int LDS_PAD_FLOATS = 24 * 1024;
static_assert(L_END <= LDS_PAD_FLOATS, "lds");

using u32x4 = __attribute__((ext_vector_type(4))) unsigned;
using u32x2 = __attribute__((ext_vector_type(2))) unsigned;
using f32x4 = __attribute__((ext_vector_type(4))) float;
using f32x2 = __attribute__((ext_vector_type(2))) float;
using bf16x2 = __attribute__((ext_vector_type(2))) __bf16;
typedef unsigned long long u64;

__device__ __forceinline__ float dot2(unsigned k, unsigned q, float acc) {
  return __builtin_amdgcn_fdot2_f32_bf16(__builtin_bit_cast(bf16x2, k), __builtin_bit_cast(bf16x2, q), acc, false);
}
__device__ __forceinline__ unsigned cvtpk(float lo, float hi) {
  unsigned r; asm volatile("v_cvt_pk_bf16_f32 %0, %1, %2" : "=v"(r) : "v"(lo), "v"(hi)); return r;
}
template <int CTRL> __device__ __forceinline__ float dpp(float x) {
  return __builtin_bit_cast(float, __builtin_amdgcn_mov_dpp(__builtin_bit_cast(int, x), CTRL, 0xf, 0xf, true));
}
constexpr int XOR1 = 0xB1, XOR2 = 0x4E, XOR7 = 0x141, XOR8 = 0x128, ROR4 = 0x124, BC0 = 0x150;
__device__ __forceinline__ float xrow16_max(float x) {
  auto s = __builtin_amdgcn_permlane16_swap(__float_as_uint(x), __float_as_uint(x), false, false);
  x = fmaxf(__uint_as_float(s[0]), __uint_as_float(s[1]));
  auto t = __builtin_amdgcn_permlane32_swap(__float_as_uint(x), __float_as_uint(x), false, false);
  return fmaxf(__uint_as_float(t[0]), __uint_as_float(t[1]));
}
__device__ __forceinline__ float xrow16_sum(float x) {
  auto s = __builtin_amdgcn_permlane16_swap(__float_as_uint(x), __float_as_uint(x), false, false);
  x = __uint_as_float(s[0]) + __uint_as_float(s[1]);
  auto t = __builtin_amdgcn_permlane32_swap(__float_as_uint(x), __float_as_uint(x), false, false);
  return __uint_as_float(t[0]) + __uint_as_float(t[1]);
}
__device__ __forceinline__ float bflo(unsigned v) { return __uint_as_float(v << 16); }
__device__ __forceinline__ float bfhi(unsigned v) { return __uint_as_float(v & 0xffff0000u); }
__device__ __forceinline__ void st_gran(u64* p, unsigned data, unsigned tag) {
  __hip_atomic_store(p, ((u64)tag << 32) | (u64)data, __ATOMIC_RELAXED, __HIP_MEMORY_SCOPE_AGENT);
}
__device__ __forceinline__ u64 ld_gran(const u64* p) {
  return __hip_atomic_load(p, __ATOMIC_RELAXED, __HIP_MEMORY_SCOPE_AGENT);
}
__device__ __forceinline__ void wg_barrier() {
  asm volatile("s_waitcnt lgkmcnt(0)" ::: "memory");
  __builtin_amdgcn_s_barrier();
  asm volatile("" ::: "memory");
}
typedef __amdgpu_buffer_rsrc_t rsrc_t;
__device__ __forceinline__ u32x4 ld_kv(rsrc_t r, unsigned voff, unsigned soff) {
  return __builtin_amdgcn_raw_buffer_load_b128(r, voff + soff, 0, KV_NT ? 2 : 0);
}
#define STAMP(i) do { } while (0)
struct Tile { u32x4 k[NI]; u32x4 v[NI]; };

__device__ __forceinline__ void issue(Tile& t, rsrc_t kr, rsrc_t vr, unsigned voff, unsigned soff) {
#pragma unroll
  for (int i = 0; i < NI; ++i) t.k[i] = ld_kv(kr, voff + i * 1024, soff);
#pragma unroll
  for (int i = 0; i < NI; ++i) t.v[i] = ld_kv(vr, voff + i * 1024, soff);
}
template <int LO, int HI> __device__ __forceinline__ void issue_units(Tile& a, Tile& b, rsrc_t kr, rsrc_t vr, unsigned voff, unsigned soff) {
#pragma unroll
  for (int i = 0; i < NI; ++i) if (LO <= 0 && 0 < HI) a.k[i] = ld_kv(kr, voff + i * 1024, soff);
#pragma unroll
  for (int i = 0; i < NI; ++i) if (LO <= 1 && 1 < HI) a.v[i] = ld_kv(vr, voff + i * 1024, soff);
#pragma unroll
  for (int i = 0; i < NI; ++i) if (LO <= 2 && 2 < HI) b.k[i] = ld_kv(kr, voff + i * 1024, soff + 64 * TBYTES);
#pragma unroll
  for (int i = 0; i < NI; ++i) if (LO <= 3 && 3 < HI) b.v[i] = ld_kv(vr, voff + i * 1024, soff + 64 * TBYTES);
}
extern "C" __device__ int llvm_raw_atomic_add(int, rsrc_t, int, int, int) __asm("llvm.amdgcn.raw.ptr.buffer.atomic.add.i32");
__device__ __forceinline__ void refill(Tile& t, rsrc_t kr, rsrc_t vr, unsigned voff, unsigned tkt, bool req, unsigned& tk, rsrc_t cr, int lane) {
  tk = tkt + 128;
  const bool big = tkt < NBIG;
  const unsigned lo = voff + (big ? tkt * TBYTES : NBIG * TBYTES + (tkt - NBIG) * (TBYTES / 2));
  const unsigned hi = big ? lo : 0x40000000u;
  __builtin_amdgcn_sched_barrier(0);
#pragma unroll
  for (int i = 0; i < NI; ++i) t.k[i] = ld_kv(kr, (i < NI / 2 ? lo : hi) + i * 1024, 0);
#pragma unroll
  for (int i = 0; i < NI; ++i) t.v[i] = ld_kv(vr, (i < NI / 2 ? lo : hi) + i * 1024, 0);
}

template <int J> __device__ __forceinline__ float bcast(float p) { return dpp<BC0 + J>(p); }

__device__ __forceinline__ void compute(const Tile& t, const u32x4& q0, const u32x4& q1, int c, bool half, float& m, float& l, f32x2 (&acc)[8]) {
  float x[2 * NI];
#pragma unroll
  for (int i = 0; i < NI; ++i) {
    float d0 = 0.f, d1 = 0.f;
#pragma unroll
    for (int k = 0; k < 4; ++k) { d0 = dot2(t.k[i][k], q0[k], d0); d1 = dot2(t.k[i][k], q1[k], d1); }
    x[2 * i] = d0; x[2 * i + 1] = d1;
  }
#define TR_STEP(HALF, CTRL, BIT) _Pragma("unroll") for (int h = 0; h < (HALF); ++h) { \
    const float lo_ = x[h], hi_ = x[h + (HALF)]; const float t1_ = lo_ + dpp<CTRL>(lo_), t2_ = hi_ + dpp<CTRL>(hi_); \
    x[h] = (c & (BIT)) ? t2_ : t1_; }
  TR_STEP(8, XOR8, 8) TR_STEP(4, XOR7, 4) TR_STEP(2, XOR2, 2) TR_STEP(1, XOR1, 1)
  constexpr int SH = 0, HX = XOR2, A0 = 0xA0, A1 = 0xF5;
#undef TR_STEP
  const float sc = (half && (c & 8)) ? -INFINITY : x[0] * C2;
  float pm = NI == 8 ? fmaxf(sc, dpp<HX>(sc)) : sc;
  pm = fmaxf(pm, dpp<ROR4>(pm)); pm = fmaxf(pm, dpp<XOR8>(pm)); pm = xrow16_max(pm);
  const float mn = fmaxf(m, pm);
  const float alpha = __builtin_amdgcn_exp2f(m - mn), p = __builtin_amdgcn_exp2f(sc - mn);
  l = fmaf(l, alpha, p); m = mn;
  const float a0 = dpp<A0>(alpha), a1 = dpp<A1>(alpha);
#pragma unroll
  for (int k = 0; k < 4; ++k) { acc[k] *= a0; acc[4 + k] *= a1; }
  float pb[16];
  pb[0] = bcast<0 << SH>(p); pb[1] = bcast<1 << SH>(p); pb[2] = bcast<2 << SH>(p); pb[3] = bcast<3 << SH>(p);
  pb[4] = bcast<4 << SH>(p); pb[5] = bcast<5 << SH>(p); pb[6] = bcast<6 << SH>(p); pb[7] = bcast<7 << SH>(p);
  pb[8] = bcast<8>(p); pb[9] = bcast<9>(p); pb[10] = bcast<10>(p); pb[11] = bcast<11>(p);
  pb[12] = bcast<12>(p); pb[13] = bcast<13>(p); pb[14] = bcast<14>(p); pb[15] = bcast<15>(p);
#pragma unroll
  for (int i = 0; i < NI; ++i) {
#pragma unroll
    for (int k = 0; k < 4; ++k) {
      const f32x2 vf = {bflo(t.v[i][k]), bfhi(t.v[i][k])};
      const f32x2 p0 = {pb[2 * i], pb[2 * i]}, p1 = {pb[2 * i + 1], pb[2 * i + 1]};
      acc[k] = __builtin_elementwise_fma(p0, vf, acc[k]);
      acc[4 + k] = __builtin_elementwise_fma(p1, vf, acc[4 + k]);
    }
  }
}
__device__ __forceinline__ void touch(const Tile& t) { asm volatile("" : : "v"(t.v[NI - 1])); }

#define SB() __builtin_amdgcn_sched_barrier(0)
template <bool W0>
__device__ __forceinline__ unsigned stream_body(const unsigned short* __restrict__ x, const u32x4* __restrict__ kc, const u32x4* __restrict__ vc,
                                                const unsigned short* __restrict__ wq, const u32x4* __restrict__ wo, u64* qg, unsigned* ep, unsigned* cnt, unsigned char* ws,
                                                float* lds, int lane, int wave, int b, int kvh, int s, int group,
                                                float& m, float& l, f32x2 (&acc)[8]) {
  const int c = lane & 15;
  unsigned tag = 0;
  if (W0) tag = __hip_atomic_load(ep, __ATOMIC_RELAXED, __HIP_MEMORY_SCOPE_AGENT) + 1u;
  const int rg = lane >> 3, cq = lane & 7;
  u32x2 wqv[9]; unsigned short xv[9];
#pragma unroll
  for (int i = 0; i < 9; ++i) xv[i] = x[b * DM + 72 * wave + 8 * i + rg];
#pragma unroll
  for (int i = 0; i < 9; ++i)
    wqv[i] = *reinterpret_cast<const u32x2*>(wq + (size_t)(72 * wave + 8 * i + rg) * QCOLS + kvh * 256 + 32 * s + 4 * cq);
  SB();
  const rsrc_t kr = __builtin_amdgcn_make_buffer_rsrc((void*)(kc + (size_t)group * SK * (DH / 8)), 0, SK * DH * 2, 0x00020000);
  const rsrc_t vr = __builtin_amdgcn_make_buffer_rsrc((void*)(vc + (size_t)group * SK * (DH / 8)), 0, SK * DH * 2, 0x00020000);
  const unsigned voff = lane * 16, soff0 = (s * WAVES + wave) * TBYTES;
  Tile A, Bt;
  if (!W0) { issue_units<0, H1>(A, Bt, kr, vr, voff, soff0); SB(); }
  {
#pragma unroll
    for (int i = 0; i < 9; ++i) asm volatile("" : "+v"(wqv[i]));
    float a0 = 0.f, a1 = 0.f, a2 = 0.f, a3 = 0.f;
#pragma unroll
    for (int i = 0; i < 9; ++i) {
      const float xf = __uint_as_float((unsigned)xv[i] << 16);
      a0 = fmaf(xf, bflo(wqv[i][0]), a0); a1 = fmaf(xf, bfhi(wqv[i][0]), a1);
      a2 = fmaf(xf, bflo(wqv[i][1]), a2); a3 = fmaf(xf, bfhi(wqv[i][1]), a3);
    }
    a0 += dpp<XOR8>(a0); a1 += dpp<XOR8>(a1); a2 += dpp<XOR8>(a2); a3 += dpp<XOR8>(a3);
    a0 = xrow16_sum(a0); a1 = xrow16_sum(a1); a2 = xrow16_sum(a2); a3 = xrow16_sum(a3);
    if (lane < 8) *reinterpret_cast<f32x4*>(lds + L_QP + wave * 32 + 4 * lane) = f32x4{a0, a1, a2, a3};
  }
  wg_barrier();
  if (W0) {
    STAMP(6);
    if (s == 0) {
      if (lane == 0) __hip_atomic_store(cnt, 256u, __ATOMIC_RELAXED, __HIP_MEMORY_SCOPE_AGENT);
      asm volatile("s_waitcnt vmcnt(0)" ::: "memory");
    }
    if (lane < 16) {
      float q0 = 0.f, q1 = 0.f;
#pragma unroll
      for (int w = 0; w < WAVES; ++w) { const f32x2 v = *reinterpret_cast<const f32x2*>(lds + L_QP + w * 32 + 2 * lane); q0 += v[0]; q1 += v[1]; }
      st_gran(qg + 16 * s + lane, cvtpk(q0, q1), tag);
    }
    if (lane == 0) reinterpret_cast<unsigned*>(lds)[L_TAG] = tag;
    u64 g0, g1;
#pragma unroll 1
    for (unsigned it = 0; it < SPIN_MAX; ++it) {
      g0 = ld_gran(qg + lane); g1 = ld_gran(qg + 64 + lane);
      const bool ok = (unsigned)(g0 >> 32) == tag && (unsigned)(g1 >> 32) == tag;
      if (__all(ok)) break;
      __builtin_amdgcn_s_sleep(1);
    }
    reinterpret_cast<unsigned*>(lds)[L_Q + lane] = (unsigned)g0;
    reinterpret_cast<unsigned*>(lds)[L_Q + 64 + lane] = (unsigned)g1;
    STAMP(7);
  }
  if (!W0) { issue_units<H1, H2>(A, Bt, kr, vr, voff, soff0); SB(); }
  wg_barrier();
  if (W0) issue_units<0, 4>(A, Bt, kr, vr, voff, soff0); else issue_units<H2, 4>(A, Bt, kr, vr, voff, soff0);
  SB();
  u32x4 wov[5];
  const u32x4* wop = wo + (size_t)(kvh * 256 + 32 * s) * (DM / 8);
#pragma unroll
  for (int i = 0; i < 5; ++i) { const int ch = (i < 4 || wave < 4) ? wave + 8 * i : wave; wov[i] = wop[ch * 64 + lane]; }
  SB();
  const u32x4 q0 = *reinterpret_cast<const u32x4*>(lds + L_Q + 4 * c);
  const u32x4 q1 = *reinterpret_cast<const u32x4*>(lds + L_Q + 64 + 4 * c);
  tag = reinterpret_cast<const unsigned*>(lds)[L_TAG];
  STAMP(1);
  SB();
  unsigned tka = 128 + s * WAVES + wave, tkb = 192 + s * WAVES + wave;
  const rsrc_t cr = __builtin_amdgcn_make_buffer_rsrc((void*)cnt, 0, 4, 0x00020000);
  bool va = true, vb = true, ha = false, hb = false;
#pragma unroll 1
  do {
    if (va) compute(A, q0, q1, c, ha, m, l, acc); else touch(A);
    SB();
    unsigned t = va ? __builtin_amdgcn_readfirstlane(tka) : NT;
    va = t < NT; ha = t >= NBIG;
    refill(A, kr, vr, voff, t, va, tka, cr, lane);
    SB();
    if (vb) compute(Bt, q0, q1, c, hb, m, l, acc); else touch(Bt);
    SB();
    t = vb ? __builtin_amdgcn_readfirstlane(tkb) : NT;
    vb = t < NT; hb = t >= NBIG;
    refill(Bt, kr, vr, voff, t, vb, tkb, cr, lane);
    SB();
  } while (va || vb);
#pragma unroll
  for (int i = 0; i < 5; ++i) { const int ch = (i < 4 || wave < 4) ? wave + 8 * i : wave;
    *reinterpret_cast<u32x4*>(lds + L_WO + ch * 256 + lane * 4) = wov[i]; }
  return tag;
}

__global__ void __launch_bounds__(NTHR, 2)
gqa_decode_kernel(const unsigned short* __restrict__ x, const u32x4* __restrict__ kc, const u32x4* __restrict__ vc,
                  const unsigned short* __restrict__ wq, const u32x4* __restrict__ wo, unsigned short* __restrict__ out,
                  unsigned char* __restrict__ ws) {
  __shared__ __attribute__((aligned(16))) float lds[LDS_PAD_FLOATS];
  const int tid = threadIdx.x, lane = tid & 63, wave = __builtin_amdgcn_readfirstlane(tid >> 6), c = lane & 15, r = lane >> 4;
  const int bid = blockIdx.x;
  const int j = bid >> 3, kvh = (bid & 7) + 8 * (j & 1), b = (j >> 1) & 1, s = j >> 2, group = b * HKV + kvh;
  unsigned* const cnt = reinterpret_cast<unsigned*>(ws + WS_CNT + group * 256);
  u64* const qg = reinterpret_cast<u64*>(ws + WS_Q) + group * 128;
  u64* const pg = reinterpret_cast<u64*>(ws + WS_P) + (size_t)group * 8 * 2 * PSTR;
  u64* const yg = reinterpret_cast<u64*>(ws + WS_Y);
  unsigned* const ep = reinterpret_cast<unsigned*>(ws + WS_EPOCH);

  STAMP(0);
  float m = -INFINITY, l = 0.f;
  f32x2 acc[8];
#pragma unroll
  for (int k = 0; k < 8; ++k) acc[k] = f32x2{0.f, 0.f};
  unsigned tag;
  if (wave == 0) tag = stream_body<true>(x, kc, vc, wq, wo, qg, ep, cnt, ws, lds, lane, wave, b, kvh, s, group, m, l, acc);
  else tag = stream_body<false>(x, kc, vc, wq, wo, qg, ep, cnt, ws, lds, lane, wave, b, kvh, s, group, m, l, acc);

#pragma unroll
  for (int k = 0; k < 8; ++k) { acc[k][0] = xrow16_sum(acc[k][0]); acc[k][1] = xrow16_sum(acc[k][1]); }
  if (NI == 8) l += dpp<XOR2>(l);
  l += dpp<ROR4>(l); l += dpp<XOR8>(l); l = xrow16_sum(l);
  if (r == 0) {
    float* rec = lds + L_MRG + wave * 2 * MREC;
    *reinterpret_cast<f32x4*>(rec + 4 + 8 * c) = f32x4{acc[0][0], acc[0][1], acc[1][0], acc[1][1]};
    *reinterpret_cast<f32x4*>(rec + 8 + 8 * c) = f32x4{acc[2][0], acc[2][1], acc[3][0], acc[3][1]};
    *reinterpret_cast<f32x4*>(rec + MREC + 4 + 8 * c) = f32x4{acc[4][0], acc[4][1], acc[5][0], acc[5][1]};
    *reinterpret_cast<f32x4*>(rec + MREC + 8 + 8 * c) = f32x4{acc[6][0], acc[6][1], acc[7][0], acc[7][1]};
    if (NI == 8 ? c < 2 : (c == 0 || c == 2)) { const int g = NI == 8 ? c : c >> 1; rec[g * MREC] = m; rec[g * MREC + 1] = l; }
  }
  STAMP(2);
  wg_barrier();
  STAMP(3);
  if (tid < 260) {
    const int g = tid < 256 ? tid >> 7 : (tid >> 1) & 1, d = tid & 127;
    const float* rec = lds + L_MRG + g * MREC;
    float M = -INFINITY;
#pragma unroll
    for (int w = 0; w < WAVES; ++w) M = fmaxf(M, rec[w * 2 * MREC]);
    float o = 0.f;
    const int idx = tid < 256 ? 4 + d : 1;
#pragma unroll
    for (int w = 0; w < WAVES; ++w) o = fmaf(__builtin_amdgcn_exp2f(rec[w * 2 * MREC] - M), rec[w * 2 * MREC + idx], o);
    u64* dst = pg + (s * 2 + g) * PSTR;
    if (tid < 256) st_gran(dst + d, __float_as_uint(o), tag);
    else st_gran(dst + 128 + (tid & 1), __float_as_uint((tid & 1) ? o : M), tag);
  }
  if (wave == 0) {
    const int g = s >> 2, dq = (s & 3) * 32, h = lane >> 5, dd = lane & 31;
    const u64* src = pg + g * PSTR + (size_t)(4 * h) * 2 * PSTR + dq + dd;
    const u64* msrc = pg + g * PSTR + (size_t)((lane & 15) >> 1) * 2 * PSTR + 128 + (lane & 1);
    u64 v0, v1, v2, v3, ml;
#pragma unroll 1
    for (unsigned it = 0; it < SPIN_MAX; ++it) {
      v0 = ld_gran(src); v1 = ld_gran(src + 2 * PSTR); v2 = ld_gran(src + 4 * PSTR); v3 = ld_gran(src + 6 * PSTR); ml = ld_gran(msrc);
      const bool ok = (unsigned)(v0 >> 32) == tag && (unsigned)(v1 >> 32) == tag && (unsigned)(v2 >> 32) == tag &&
                      (unsigned)(v3 >> 32) == tag && (unsigned)(ml >> 32) == tag;
      if (__all(ok)) break;
      __builtin_amdgcn_s_sleep(2);
    }
    const float mlv = __uint_as_float((unsigned)ml);
    const float Ms = dpp<0xA0>(mlv), Ls = dpp<0xF5>(mlv);
    float Mx = fmaxf(Ms, dpp<XOR2>(Ms)); Mx = fmaxf(Mx, dpp<ROR4>(Mx)); Mx = fmaxf(Mx, dpp<XOR8>(Mx));
    const float wgt = __builtin_amdgcn_exp2f(Ms - Mx);
    float Lt = wgt * Ls; Lt += dpp<XOR2>(Lt); Lt += dpp<ROR4>(Lt); Lt += dpp<XOR8>(Lt);
    const float w0 = __shfl(wgt, 8 * h + 0), w1 = __shfl(wgt, 8 * h + 2), w2 = __shfl(wgt, 8 * h + 4), w3 = __shfl(wgt, 8 * h + 6);
    float o = w0 * __uint_as_float((unsigned)v0);
    o = fmaf(w1, __uint_as_float((unsigned)v1), o); o = fmaf(w2, __uint_as_float((unsigned)v2), o); o = fmaf(w3, __uint_as_float((unsigned)v3), o);
    o += __shfl_xor(o, 32);
    o = o / Lt;
    if (lane < 32) lds[L_O + lane] = bflo(cvtpk(o, o) & 0xffffu);
  }
  wg_barrier();
  STAMP(4);
  if (tid < DM / 2) {
    float y0 = 0.f, y1 = 0.f;
    const unsigned* wl = reinterpret_cast<const unsigned*>(lds) + L_WO + tid;
#pragma unroll
    for (int q4 = 0; q4 < 8; ++q4) {
      const f32x4 ov = *reinterpret_cast<const f32x4*>(lds + L_O + 4 * q4);
#pragma unroll
      for (int e = 0; e < 4; ++e) { const unsigned wv = wl[(4 * q4 + e) * (DM / 2)]; y0 = fmaf(ov[e], bflo(wv), y0); y1 = fmaf(ov[e], bfhi(wv), y1); }
    }
    u64* dst = yg + ((size_t)b * 128 + kvh * 8 + s) * DM + 2 * tid;
    st_gran(dst, __float_as_uint(y0), tag);
    st_gran(dst + 1, __float_as_uint(y1), tag);
  }
  if (bid < NRED) {
    const int p = tid >> 2, cc = tid & 3;
    const u64* s0 = yg + (size_t)p * DM + 4 * bid + cc;
    const u64* s1 = s0 + (size_t)128 * DM;
    u64 y0, y1;
#pragma unroll 1
    for (unsigned it = 0; it < SPIN_MAX; ++it) {
      y0 = ld_gran(s0); y1 = ld_gran(s1);
      const bool ok = (unsigned)(y0 >> 32) == tag && (unsigned)(y1 >> 32) == tag;
      if (__all(ok)) break;
      __builtin_amdgcn_s_sleep(4);
    }
    float f0 = __uint_as_float((unsigned)y0), f1 = __uint_as_float((unsigned)y1);
    f0 += dpp<ROR4>(f0); f0 += dpp<XOR8>(f0); f0 = xrow16_sum(f0);
    f1 += dpp<ROR4>(f1); f1 += dpp<XOR8>(f1); f1 = xrow16_sum(f1);
    if (lane < 4) { lds[L_RED + wave * 8 + lane] = f0; lds[L_RED + wave * 8 + 4 + lane] = f1; }
    wg_barrier();
    if (tid < 8) {
      float t = 0.f;
#pragma unroll
      for (int w = 0; w < WAVES; ++w) t += lds[L_RED + w * 8 + tid];
      out[(tid >> 2) * DM + 4 * bid + (tid & 3)] = (unsigned short)(cvtpk(t, t) & 0xffffu);
    }
    if (bid == 0 && tid == 0) __hip_atomic_store(ep, tag, __ATOMIC_RELAXED, __HIP_MEMORY_SCOPE_AGENT);
  }
  STAMP(5);
}
}

extern "C" void kernel_launch(void* const* d_in, const int* in_sizes, int n_in, void* d_out, int out_size, void* d_ws, size_t ws_size,
                              hipStream_t stream) {
  (void)in_sizes; (void)n_in; (void)out_size; (void)ws_size;
  gqa_decode_kernel<<<NWG, NTHR, 0, stream>>>((const unsigned short*)d_in[0], (const u32x4*)d_in[1], (const u32x4*)d_in[2],
                                              (const unsigned short*)d_in[3], (const u32x4*)d_in[4], (unsigned short*)d_out,
                                              (unsigned char*)d_ws);
}
```
